```python
import torch

def _softmax(x, axis=-1):
    return torch.softmax(x, dim=axis)
def _gelu(x):
    return torch.nn.functional.gelu(x, approximate="tanh")
def _silu(x):
    return torch.nn.functional.silu(x)
def _sigmoid(x):
    return torch.sigmoid(x)
def _layernorm(x, axis=-1, eps=1e-5):
    m = x.mean(dim=axis, keepdim=True)
    v = x.var(dim=axis, keepdim=True, unbiased=False)
    return (x - m) / torch.sqrt(v + eps)
def _scatter_add(tgt, idx, src, axis=0):
    return tgt.clone().index_add_(axis, idx, src)
def _segment_sum(src, seg, n):
    out = torch.zeros((n,) + src.shape[1:], dtype=src.dtype)
    return out.index_add_(0, seg, src)
def _pad(x, pw):
    flat = tuple(v for pair in reversed(pw) for v in pair)
    return torch.nn.functional.pad(x, flat)

kernel_name = 'standalone__channel_attn_cbam__vision'

def setup_inputs():

    _w = (lambda a: (lambda t: t.to(torch.bfloat16) if t.is_floating_point() else t)(torch.as_tensor(a)))
    H = W = 256
    C = 384
    r = max(C // 4, 1)
    return {
        "x": _w((lambda *s: torch.randn(*s, dtype=torch.bfloat16) * 0.1)(1, H, W, C) + (lambda *s: torch.randn(*s, dtype=torch.bfloat16) * 0.1)(1, 1, 1, C) * 2.0),
        "w1": _w((lambda *s: torch.randn(*s, dtype=torch.bfloat16) * 0.1)(C, r)),
        "w2": _w((lambda *s: torch.randn(*s, dtype=torch.bfloat16) * 0.1)(r, C)),
    }

def reference(x, w1, w2):
    B, H, W, C = x.shape
    xf = x.reshape(B, H * W, C)
    gap = torch.mean(xf, 1)
    gmp = torch.amax(xf, 1)
    h_a = gap @ w1
    h_a = h_a * (lambda x, dt: x.to(dt))(h_a > 0, h_a.dtype)
    h_m = gmp @ w1
    h_m = h_m * (lambda x, dt: x.to(dt))(h_m > 0, h_m.dtype)
    scale = _sigmoid(h_a @ w2 + h_m @ w2)
    return x * scale[:, None, None, :]
```

```cpp
#include <hip/hip_runtime.h>
#include <hip/hip_bf16.h>
#include <stdint.h>

typedef unsigned u32;
typedef unsigned long long u64;
typedef u32 v3u __attribute__((ext_vector_type(3)));
typedef u32 v4u __attribute__((ext_vector_type(4)));
typedef u32 v32u __attribute__((ext_vector_type(32)));
typedef float v2f __attribute__((ext_vector_type(2)));
typedef float v4f __attribute__((ext_vector_type(4)));
typedef short s16x4 __attribute__((ext_vector_type(4)));
typedef short s16x8 __attribute__((ext_vector_type(8)));
#define LAS __attribute__((address_space(3)))
#define GAS __attribute__((address_space(1)))

static constexpr int C = 384, HID = 96, NPOS = 65536;
static constexpr int NWG = 256, T = 512;
static constexpr int SC = 192;
static constexpr int NRG = 128;
static constexpr int ROWB = 768;
static constexpr int RGB = 512 * ROWB;
static constexpr int KSTEP = 16 * ROWB;
static constexpr int NRED = 12;
static constexpr int AUX_NT = 2, AUX_SC1 = 16;
static constexpr int GRP = 8;
static constexpr int PACE = 75;
static constexpr u32 WS_MB2 = 4096;
static constexpr u32 WS_MB1 = 4096 + 20480;
static constexpr u32 WS_END = WS_MB1 + NRED * NRG * 32 * 16;
static constexpr int P1 = 192, P2 = 384;
static constexpr int L_W2 = 0;
static constexpr int L_W1 = L_W2 + HID * P2;
static constexpr int L_RS = L_W1 + 32 * P1;
static constexpr int L_RM = L_RS + 8 * SC * 4;
static constexpr int L_H = L_RM + 8 * SC * 4;
static constexpr int L_SC = L_H + 640;
static constexpr int L_RR = L_SC + SC * 4;
static constexpr int L_GA = L_RR + 8 * 32 * 8;
static constexpr int L_USED = L_GA + 6 * 192;
static constexpr int L_END = 84 * 1024;
static_assert(L_USED <= L_END, "lds map");
static constexpr u32 kSpinCap = 1u << 18;

__device__ __forceinline__ float bflo(u32 u) { return __uint_as_float(u << 16); }
__device__ __forceinline__ float bfhi(u32 u) { return __uint_as_float(u & 0xffff0000u); }
__device__ __forceinline__ u32 cvtpk(float lo, float hi) {
    typedef __bf16 bf2 __attribute__((ext_vector_type(2)));
    v2f v = {lo, hi};
    return __builtin_bit_cast(u32, __builtin_convertvector(v, bf2));
}
__device__ __forceinline__ float rbf(float f) { return bflo(cvtpk(f, f)); }
__device__ __forceinline__ float max3(float a, float b, float c) { return __builtin_fmaxf(__builtin_fmaxf(a, b), c); }
__device__ __forceinline__ float vmax(float a, float b) { float r; asm("v_max_f32 %0, %1, %2" : "=v"(r) : "v"(a), "v"(b)); return r; }
__device__ __forceinline__ float vmax3(float a, float b, float c) { float r; asm("v_max3_f32 %0, %1, %2, %3" : "=v"(r) : "v"(a), "v"(b), "v"(c)); return r; }
__device__ __forceinline__ float vmax8(float a, float b, float c, float d, float e, float f, float g, float h) { return vmax(vmax3(d, e, f), vmax3(g, h, vmax3(a, b, c))); }
__device__ __forceinline__ u64 now() { return __builtin_amdgcn_s_memrealtime(); }
__device__ __forceinline__ float add16(float v) { auto a = __builtin_amdgcn_permlane16_swap(__float_as_uint(v), __float_as_uint(v), false, false); return __uint_as_float(a[0]) + __uint_as_float(a[1]); }
__device__ __forceinline__ float add32(float v) { auto a = __builtin_amdgcn_permlane32_swap(__float_as_uint(v), __float_as_uint(v), false, false); return __uint_as_float(a[0]) + __uint_as_float(a[1]); }
__device__ __forceinline__ float max32(float v) { auto a = __builtin_amdgcn_permlane32_swap(__float_as_uint(v), __float_as_uint(v), false, false); return vmax(__uint_as_float(a[0]), __uint_as_float(a[1])); }

#define PLD(i) "s_mov_b64 exec, %[m" #i "]\n\ts_cbranch_execz .Lpoll%=_" #i "\n\tglobal_load_dwordx4 %[g" #i "], %[vo], %[b] sc1\n.Lpoll%=_" #i ":\n\t"
#define PADV "s_mov_b64 exec, %[sv]\n\tv_add_u32 %[vo], %[st], %[vo]\n\t"
template <int STRIDE>
__device__ __forceinline__ void repoll3(const char* base, u32 off, const u64 (&m)[3], v4u (&g)[3]) {
    u64 sv;
    asm volatile("s_mov_b64 %[sv], exec\n\t" PLD(0) PADV PLD(1) PADV PLD(2) "s_mov_b64 exec, %[sv]\n\ts_waitcnt vmcnt(0)"
                 : [sv] "=&s"(sv), [vo] "+v"(off), [g0] "+v"(g[0]), [g1] "+v"(g[1]), [g2] "+v"(g[2])
                 : [m0] "s"(m[0]), [m1] "s"(m[1]), [m2] "s"(m[2]), [b] "s"(base), [st] "i"(STRIDE) : "memory");
}
template <int STRIDE>
__device__ __forceinline__ void repoll8(const char* base, u32 off, const u64 (&m)[8], v4u (&g)[8]) {
    u64 sv;
    asm volatile("s_mov_b64 %[sv], exec\n\t" PLD(0) PADV PLD(1) PADV PLD(2) PADV PLD(3) PADV PLD(4) PADV PLD(5) PADV PLD(6) PADV PLD(7) "s_mov_b64 exec, %[sv]\n\ts_waitcnt vmcnt(0)"
                 : [sv] "=&s"(sv), [vo] "+v"(off), [g0] "+v"(g[0]), [g1] "+v"(g[1]), [g2] "+v"(g[2]), [g3] "+v"(g[3]), [g4] "+v"(g[4]), [g5] "+v"(g[5]), [g6] "+v"(g[6]), [g7] "+v"(g[7])
                 : [m0] "s"(m[0]), [m1] "s"(m[1]), [m2] "s"(m[2]), [m3] "s"(m[3]), [m4] "s"(m[4]), [m5] "s"(m[5]), [m6] "s"(m[6]), [m7] "s"(m[7]), [b] "s"(base), [st] "i"(STRIDE) : "memory");
}
__device__ __forceinline__ void glds16(const char* base, u32 off, u32 lds_base) {
    u32 sv; asm volatile("s_mov_b32 %0, m0\n\ts_mov_b32 m0, %3\n\ts_nop 0\n\tglobal_load_lds_dwordx4 %1, %2\n\ts_mov_b32 m0, %0" : "=&s"(sv) : "v"(off), "s"(base), "s"(lds_base) : "memory");
}

__global__ __launch_bounds__(T, 2) void cbam_kernel(const char* x, const char* w1, const char* w2, char* out, char* ws) {
    __shared__ __attribute__((aligned(16))) char lds[L_END];
    const int t = threadIdx.x, lane = t & 63;
    const int w = __builtin_amdgcn_readfirstlane(t >> 6);
    const int b = blockIdx.x;
    const int slice = (b >> 3) & 1, rg = (b & 7) | ((b >> 4) << 3);
    const bool isred = b < NRED;
    asm volatile("" : "+s"(x), "+s"(w1), "+s"(w2), "+s"(out), "+s"(ws));

    const size_t rbase = (size_t)rg * RGB;
    const __amdgpu_buffer_rsrc_t rx = __builtin_amdgcn_make_buffer_rsrc((void*)(x + rbase + slice * 384), 0, RGB - slice * 384, 0x00020000);
    const __amdgpu_buffer_rsrc_t ro = __builtin_amdgcn_make_buffer_rsrc((void*)(out + rbase + slice * 384), 0, RGB - slice * 384, 0x00020000);
    const __amdgpu_buffer_rsrc_t rws = __builtin_amdgcn_make_buffer_rsrc((void*)ws, 0, WS_END, 0x00020000);
    const __amdgpu_buffer_rsrc_t rw1 = __builtin_amdgcn_make_buffer_rsrc((void*)w1, 0, C * HID * 2, 0x00020000);

    const int hrow = lane >> 5, j = lane & 31;
    const u32 voff = (u32)((2 * w + hrow) * ROWB + 12 * j);

    v32u X[4];
    u32 touch;
    v4u wr1 = {0u, 0u, 0u, 0u};
#pragma unroll
    for (int k = 0; k < 32; ++k) {
        const v3u ld = __builtin_amdgcn_raw_buffer_load_b96(rx, voff, k * KSTEP, AUX_NT);
        X[k >> 3][4 * (k & 7)] = ld[0]; X[k >> 3][4 * (k & 7) + 1] = ld[1]; X[k >> 3][4 * (k & 7) + 2] = ld[2];
        if (k % GRP == GRP - 1 && k != 31) { __builtin_amdgcn_sched_barrier(0); asm volatile("s_barrier" ::: "memory"); __builtin_amdgcn_sched_barrier(0); }
        if (k == 7) { if (isred && t < 384) wr1 = __builtin_amdgcn_raw_buffer_load_b128(rw1, (u32)(b * 6144 + t * 16), 0, 0); __builtin_amdgcn_sched_barrier(0); }
    }
    {
        const char* po = out + rbase;
        const u64 a0 = (lane & 1) ? (u64)(po + RGB - 4) : (u64)po, a1 = (lane & 1) ? (u64)(w2 + C * HID * 2 - 4) : (u64)w2, a2 = (lane & 1) ? (u64)w1 : (u64)(ws + WS_MB1);
        touch = *(const GAS u32*)((lane & 4) ? a2 : ((lane & 2) ? a1 : a0));
        __builtin_amdgcn_sched_barrier(0);
    }
    u64 epoch;
    asm volatile("s_load_dwordx2 %0, %1, 0x0\n\ts_waitcnt lgkmcnt(0)" : "=s"(epoch) : "s"(ws) : "memory");
    u64 tag = epoch * 6364136223846793005ull + 1442695040888963407ull;
    asm volatile("" : "+s"(tag));
    const u32 tagLo = (u32)tag, tagHi = (u32)(tag >> 32);

    const int q = lane >> 4, p4 = lane & 3;
    const int trow = 4 * q + ((lane & 15) >> 2);
    u32 a_part = (u32)(L_RS + (w * SC + 6 * j + 3 * hrow) * 4);
    u32 a_sum = (u32)(L_RS + t * 4);
    u32 a_mb1 = (u32)(WS_MB1 + (((slice * 6 + (t >> 5)) * NRG + rg) * 32 + (t & 31)) * 16);
    u32 a_mb2 = (u32)(WS_MB2 + (q * HID + 16 * w + (lane & 15)) * 16);
    u32 a_hw; { const int n = 16 * w + (lane & 15), kk = n & 31;
                a_hw = (u32)(L_H + ((lane & 16) ? 320 : 0) + ((n & ~31) + 8 * ((kk & 15) >> 2) + 4 * (kk >> 4) + (kk & 3)) * 2); }
    u32 a_hr = (u32)(L_H + (lane & 1) * 320 + q * 16);
    u32 a_scw = (u32)(L_SC + (32 * w + (lane & 31)) * 4), a_scr = (u32)(L_SC + 24 * j);
    asm volatile("" : "+v"(a_part), "+v"(a_sum), "+v"(a_mb1), "+v"(a_mb2), "+v"(a_hw), "+v"(a_hr), "+v"(a_scw), "+v"(a_scr));

    v2f s[3]; float m[6];
#pragma unroll
    for (int i = 0; i < 3; ++i) { s[i] = (v2f){0.f, 0.f}; m[2 * i] = -INFINITY; m[2 * i + 1] = -INFINITY; }
#pragma unroll
    for (int k = 0; k < 32; k += 2) {
#pragma unroll
        for (int i = 0; i < 3; ++i) {
            const u32 da = X[k >> 3][4 * (k & 7) + i], dc = X[k >> 3][4 * (k & 7) + 4 + i];
            const v2f a = {bflo(da), bfhi(da)}, c = {bflo(dc), bfhi(dc)};
            s[i] += a; s[i] += c;
            m[2 * i] = max3(m[2 * i], a[0], c[0]); m[2 * i + 1] = max3(m[2 * i + 1], a[1], c[1]);
        }
        asm volatile("" : "+v"(s[0]), "+v"(s[1]), "+v"(s[2]), "+v"(m[0]), "+v"(m[1]), "+v"(m[2]), "+v"(m[3]), "+v"(m[4]), "+v"(m[5]));
        if (k == 16 && isred && t < 384) {
            const int row = t / 12, c = t - row * 12;
            *(LAS v4u*)(LAS char*)(lds + L_W1 + row * P1 + (c ^ (((row >> 2) & 1) << 1)) * 16) = wr1;
        }
    }
    {
        LAS float* rs = (LAS float*)(LAS char*)(lds + a_part);
        LAS float* rm = (LAS float*)(LAS char*)(lds + a_part + (L_RM - L_RS));
#pragma unroll
        for (int i = 0; i < 3; ++i) {
            auto a = __builtin_amdgcn_permlane32_swap(__float_as_uint(s[i >> 1][i & 1]), __float_as_uint(s[(i + 3) >> 1][(i + 3) & 1]), false, false);
            auto c = __builtin_amdgcn_permlane32_swap(__float_as_uint(m[i]), __float_as_uint(m[i + 3]), false, false);
            rs[i] = __uint_as_float(a[0]) + __uint_as_float(a[1]);
            rm[i] = vmax(__uint_as_float(c[0]), __uint_as_float(c[1]));
        }
    }
    __syncthreads();
    if (t < SC) {
        const LAS float* rs = (const LAS float*)(LAS char*)(lds + a_sum);
        const LAS float* rm = (const LAS float*)(LAS char*)(lds + a_sum + (L_RM - L_RS));
        float a[8], c[8];
#pragma unroll
        for (int i = 0; i < 8; ++i) { a[i] = rs[i * SC]; c[i] = rm[i * SC]; }
        asm volatile("" : "+v"(a[0]), "+v"(a[1]), "+v"(a[2]), "+v"(a[3]), "+v"(a[4]), "+v"(a[5]), "+v"(a[6]), "+v"(a[7]),
                          "+v"(c[0]), "+v"(c[1]), "+v"(c[2]), "+v"(c[3]), "+v"(c[4]), "+v"(c[5]), "+v"(c[6]), "+v"(c[7]));
        const float ss = ((a[0] + a[1]) + (a[2] + a[3])) + ((a[4] + a[5]) + (a[6] + a[7]));
        const float mm = vmax8(c[0], c[1], c[2], c[3], c[4], c[5], c[6], c[7]);
        const v4u g = {__float_as_uint(ss), tagLo, __float_as_uint(mm), tagHi};
        __builtin_amdgcn_raw_buffer_store_b128(g, rws, a_mb1, 0, AUX_SC1);
    }

    if (isred) {
        s16x8 f1;
        {
            const int wt = w < 6 ? w : 5;
            const LAS char* b1 = (const LAS char*)(lds + L_W1 + trow * P1 + ((wt ^ ((trow >> 2) & 1)) * 32) + p4 * 8);
            const s16x4 lo = __builtin_amdgcn_ds_read_tr16_b64_v4i16((LAS s16x4*)b1);
            const s16x4 hi = __builtin_amdgcn_ds_read_tr16_b64_v4i16((LAS s16x4*)(b1 + 16 * P1));
            f1 = __builtin_shufflevector(lo, hi, 0, 1, 2, 3, 4, 5, 6, 7);
        }
        const u32 off = (u32)(WS_MB1 + b * (NRG * 32 * 16) + t * 16);
        u32 a_rrw = (u32)(L_RR + (w * 32 + j) * 8), a_rrr = (u32)(L_RR + (hrow * 128 + j) * 8);
        u32 a_gaw = (u32)(L_GA + w * 192 + hrow * 96 + (8 * ((j & 15) >> 2) + 4 * (j >> 4) + (j & 3)) * 2), a_gar = (u32)(L_GA + w * 192 + (lane & 1) * 96 + q * 16);
        u32 a_pub = (u32)(WS_MB2 + (b * HID + 16 * w + (lane & 15)) * 16);
        asm volatile("" : "+v"(a_rrw), "+v"(a_rrr), "+v"(a_gaw), "+v"(a_gar), "+v"(a_pub), "+v"(f1));
        v4u g[8]; u64 miss[8]; u32 spins = 0;
#pragma unroll
        for (int i = 0; i < 8; ++i) { g[i] = (v4u){0u, ~tagLo, 0u, 0u}; miss[i] = ~0ull; }
#pragma unroll 1
        for (;;) {
            repoll8<8192>(ws, off, miss, g);
            u64 any = 0;
#pragma unroll
            for (int i = 0; i < 8; ++i) { miss[i] = __ballot(g[i][1] != tagLo) | __ballot(g[i][3] != tagHi); any |= miss[i]; }
            if (any == 0 || ++spins > kSpinCap) break;
        }
        float ss = ((__uint_as_float(g[0][0]) + __uint_as_float(g[1][0])) + (__uint_as_float(g[2][0]) + __uint_as_float(g[3][0]))) +
                   ((__uint_as_float(g[4][0]) + __uint_as_float(g[5][0])) + (__uint_as_float(g[6][0]) + __uint_as_float(g[7][0])));
        float mm = vmax8(__uint_as_float(g[0][2]), __uint_as_float(g[1][2]), __uint_as_float(g[2][2]), __uint_as_float(g[3][2]),
                         __uint_as_float(g[4][2]), __uint_as_float(g[5][2]), __uint_as_float(g[6][2]), __uint_as_float(g[7][2]));
        ss = add32(ss); mm = max32(mm);
        if (lane < 32) *(LAS v2f*)(LAS char*)(lds + a_rrw) = (v2f){ss, mm};
        __syncthreads();
        if (w < 6) {
            const LAS v2f* pr = (const LAS v2f*)(LAS char*)(lds + a_rrr);
            const v2f v0 = pr[0], v1 = pr[32], v2 = pr[64], v3 = pr[96];
            ss = add32((v0[0] + v1[0]) + (v2[0] + v3[0]));
            mm = max32(vmax(vmax3(v0[1], v1[1], v2[1]), v3[1]));
            const float val = hrow ? mm : ss * (1.0f / NPOS);
            *(LAS unsigned short*)(LAS char*)(lds + a_gaw) = (unsigned short)(cvtpk(val, val) & 0xffffu);
            const s16x8 a = *(const LAS s16x8*)(LAS char*)(lds + a_gar);
            v4f acc = {0.f, 0.f, 0.f, 0.f};
            acc = __builtin_amdgcn_mfma_f32_16x16x32_bf16(a, f1, acc, 0, 0, 0);
            const v4u g2 = {__float_as_uint(acc[0]), tagLo, __float_as_uint(acc[1]), tagHi};
            if (lane < 16) __builtin_amdgcn_raw_buffer_store_b128(g2, rws, a_pub, 0, AUX_SC1);
        }
    }

    {
        const u32 lbase = (u32)(uintptr_t)(LAS char*)lds;
#pragma unroll
        for (int mm = 0; mm < 5; ++mm) {
            if (mm < 4 || w < 4) {
                const int i = t + 512 * mm, row = i / 24, pc = i - row * 24;
                const int c = pc ^ (((row >> 1) & 3) << 1);
                glds16(w2, (u32)(row * ROWB + slice * 384 + c * 16), lbase + L_W2 + (w * 64 + 512 * mm) * 16);
            }
        }
    }
    asm volatile("" :: "v"(touch));
#pragma unroll
    for (int g = 0; g < 4; ++g) asm volatile("" : "+v"(X[g]));
    asm volatile("s_waitcnt vmcnt(0)" ::: "memory");
    __syncthreads();
    s16x8 f2[6];
    {
        const int wt = w < 6 ? w : 5;
#pragma unroll
        for (int tl = 0; tl < 2; ++tl) {
            const LAS char* b2 = (const LAS char*)(lds + L_W2 + trow * P2 + (((2 * wt + tl) ^ ((trow >> 1) & 3)) * 32) + p4 * 8);
#pragma unroll
            for (int st = 0; st < 3; ++st) {
                const s16x4 lo = __builtin_amdgcn_ds_read_tr16_b64_v4i16((LAS s16x4*)(b2 + (32 * st) * P2));
                const s16x4 hi = __builtin_amdgcn_ds_read_tr16_b64_v4i16((LAS s16x4*)(b2 + (32 * st + 16) * P2));
                f2[tl * 3 + st] = __builtin_shufflevector(lo, hi, 0, 1, 2, 3, 4, 5, 6, 7);
            }
        }
#pragma unroll
        for (int st = 0; st < 6; ++st) asm volatile("" : "+v"(f2[st]));
    }

    v4u g[3]; u64 miss[3];
#pragma unroll
    for (int i = 0; i < 3; ++i) { g[i] = (v4u){0u, ~tagLo, 0u, 0u}; miss[i] = ~0ull; asm volatile("" : "+v"(g[i])); }
    if (w == 0) {
        const u32 so = (u32)(WS_MB2 + ((b % NRED) * HID + 80) * 16);
        u32 spins = 0;
#pragma unroll 1
        for (;;) {
            v4u sg; asm volatile("global_load_dwordx4 %0, %1, %2 sc1\n\ts_waitcnt vmcnt(0)" : "=&v"(sg) : "v"(so), "s"(ws) : "memory");
            const u64 got = ((u64)(u32)__builtin_amdgcn_readfirstlane(sg[3]) << 32) | (u32)__builtin_amdgcn_readfirstlane(sg[1]);
            if (got == tag || ++spins > kSpinCap) break;
            __builtin_amdgcn_s_sleep(1);
        }
    }
    __builtin_amdgcn_s_barrier();
    if (w < 6) {
        u32 spins = 0;
#pragma unroll 1
        for (;;) {
            repoll3<4 * HID * 16>(ws, a_mb2, miss, g);
            u64 any = 0;
#pragma unroll
            for (int i = 0; i < 3; ++i) { miss[i] = __ballot(g[i][1] != tagLo) | __ballot(g[i][3] != tagHi); any |= miss[i]; }
            if (any == 0 || ++spins > kSpinCap) break;
            __builtin_amdgcn_s_sleep(2);
        }
        float ha = (__uint_as_float(g[0][0]) + __uint_as_float(g[1][0])) + __uint_as_float(g[2][0]);
        float hm = (__uint_as_float(g[0][2]) + __uint_as_float(g[1][2])) + __uint_as_float(g[2][2]);
        ha = add32(add16(ha)); hm = add32(add16(hm));
        float hv = rbf((lane & 16) ? hm : ha);
        hv = hv * (hv > 0.f ? 1.f : 0.f);
        if (lane < 32) *(LAS unsigned short*)(LAS char*)(lds + a_hw) = (unsigned short)(cvtpk(hv, hv) & 0xffffu);
    }
    __syncthreads();
    if (w < 6) {
        const LAS char* ha = (const LAS char*)(lds + a_hr);
        v4f a0 = {0.f, 0.f, 0.f, 0.f}, a1 = {0.f, 0.f, 0.f, 0.f};
        s16x8 ah[3];
#pragma unroll
        for (int st = 0; st < 3; ++st) ah[st] = *(const LAS s16x8*)(ha + st * 64);
        __builtin_amdgcn_sched_barrier(0);
#pragma unroll
        for (int st = 0; st < 3; ++st) {
            a0 = __builtin_amdgcn_mfma_f32_16x16x32_bf16(ah[st], f2[st], a0, 0, 0, 0);
            a1 = __builtin_amdgcn_mfma_f32_16x16x32_bf16(ah[st], f2[3 + st], a1, 0, 0, 0);
        }
        const float za = (lane & 16) ? a1[0] : a0[0], zm = (lane & 16) ? a1[1] : a0[1];
        const float z = rbf(rbf(za) + rbf(zm));
        const float sg = rbf(__builtin_amdgcn_rcpf(1.0f + __expf(-z)));
        if (lane < 32) *(LAS float*)(LAS char*)(lds + a_scw) = sg;
    }
    __syncthreads();

    v2f sc[3];
    {
        const LAS v2f* sp = (const LAS v2f*)(LAS char*)(lds + a_scr);
#pragma unroll
        for (int i = 0; i < 3; ++i) sc[i] = sp[i];
    }
    const u64 t0 = now();
#pragma unroll
    for (int g4 = 0; g4 < 4; ++g4) {
#pragma unroll 1
        for (int i = 0; i < 8; ++i) {
            if ((i & 3) == 0 && (g4 | i)) { const u64 due = t0 + (u64)(((8 * g4 + i) * PACE) >> 2); while (now() < due) __builtin_amdgcn_s_sleep(1); }
            const u32 d0 = X[g4][4 * i], d1 = X[g4][4 * i + 1], d2 = X[g4][4 * i + 2];
            v3u o;
            { const v2f v = (v2f){bflo(d0), bfhi(d0)} * sc[0]; o[0] = cvtpk(v[0], v[1]); }
            { const v2f v = (v2f){bflo(d1), bfhi(d1)} * sc[1]; o[1] = cvtpk(v[0], v[1]); }
            { const v2f v = (v2f){bflo(d2), bfhi(d2)} * sc[2]; o[2] = cvtpk(v[0], v[1]); }
            __builtin_amdgcn_raw_buffer_store_b96(o, ro, voff, (g4 * 8 + i) * KSTEP, AUX_SC1);
        }
    }
    if (b == 0 && t == 0) __hip_atomic_store((u64*)ws, tag, __ATOMIC_RELAXED, __HIP_MEMORY_SCOPE_AGENT);
}

extern "C" void kernel_launch(void* const* d_in, const int* in_sizes, int n_in,
                              void* d_out, int out_size, void* d_ws, size_t ws_size,
                              hipStream_t stream) {
    cbam_kernel<<<NWG, T, 0, stream>>>((const char*)d_in[0], (const char*)d_in[1], (const char*)d_in[2], (char*)d_out, (char*)d_ws);
}
```

```cpp
#include <hip/hip_runtime.h>
#include <hip/hip_bf16.h>
#include <stdint.h>

typedef unsigned u32;
typedef unsigned long long u64;
typedef u32 v3u __attribute__((ext_vector_type(3)));
typedef u32 v4u __attribute__((ext_vector_type(4)));
typedef u32 v32u __attribute__((ext_vector_type(32)));
typedef float v2f __attribute__((ext_vector_type(2)));
typedef float v4f __attribute__((ext_vector_type(4)));
typedef short s16x4 __attribute__((ext_vector_type(4)));
typedef short s16x8 __attribute__((ext_vector_type(8)));
#define LAS __attribute__((address_space(3)))

static constexpr int C = 384, HID = 96, NPOS = 65536;
static constexpr int NWG = 256, T = 512;
static constexpr int SC = 192;
static constexpr int NRG = 128;
static constexpr int ROWB = 768;
static constexpr int RGB = 512 * ROWB;
static constexpr int KSTEP = 16 * ROWB;
static constexpr int NRED = 12;
static constexpr int AUX_NT = 2, AUX_SC1 = 16;
static constexpr int GRP = 8;
static constexpr int PACE = 75;
static constexpr u32 WS_MB2 = 4096;
static constexpr u32 WS_MB1 = 4096 + 20480;
static constexpr u32 WS_END = WS_MB1 + NRED * NRG * 32 * 16;
static constexpr int P1 = 192, P2 = 384;
static constexpr int L_W2 = 0;
static constexpr int L_W1 = L_W2 + HID * P2;
static constexpr int L_RS = L_W1 + 32 * P1;
static constexpr int L_RM = L_RS + 8 * SC * 4;
static constexpr int L_H = L_RM + 8 * SC * 4;
static constexpr int L_SC = L_H + 640;
static constexpr int L_RR = L_SC + SC * 4;
static constexpr int L_GA = L_RR + 8 * 32 * 8;
static constexpr int L_USED = L_GA + 6 * 192;
static constexpr int L_END = 84 * 1024;
static_assert(L_USED <= L_END, "lds map");
static constexpr u32 kSpinCap = 1u << 18;

__device__ __forceinline__ float bflo(u32 u) { return __uint_as_float(u << 16); }
__device__ __forceinline__ float bfhi(u32 u) { return __uint_as_float(u & 0xffff0000u); }
__device__ __forceinline__ u32 cvtpk(float lo, float hi) {
    typedef __bf16 bf2 __attribute__((ext_vector_type(2)));
    v2f v = {lo, hi};
    return __builtin_bit_cast(u32, __builtin_convertvector(v, bf2));
}
__device__ __forceinline__ float rbf(float f) { return bflo(cvtpk(f, f)); }
__device__ __forceinline__ float max3(float a, float b, float c) { return __builtin_fmaxf(__builtin_fmaxf(a, b), c); }
__device__ __forceinline__ float vmax(float a, float b) { float r; asm("v_max_f32 %0, %1, %2" : "=v"(r) : "v"(a), "v"(b)); return r; }
__device__ __forceinline__ float vmax3(float a, float b, float c) { float r; asm("v_max3_f32 %0, %1, %2, %3" : "=v"(r) : "v"(a), "v"(b), "v"(c)); return r; }
__device__ __forceinline__ float vmax8(float a, float b, float c, float d, float e, float f, float g, float h) { return vmax(vmax3(d, e, f), vmax3(g, h, vmax3(a, b, c))); }
__device__ __forceinline__ u64 now() { return __builtin_amdgcn_s_memrealtime(); }
__device__ __forceinline__ float add16(float v) { auto a = __builtin_amdgcn_permlane16_swap(__float_as_uint(v), __float_as_uint(v), false, false); return __uint_as_float(a[0]) + __uint_as_float(a[1]); }
__device__ __forceinline__ float add32(float v) { auto a = __builtin_amdgcn_permlane32_swap(__float_as_uint(v), __float_as_uint(v), false, false); return __uint_as_float(a[0]) + __uint_as_float(a[1]); }
__device__ __forceinline__ float max32(float v) { auto a = __builtin_amdgcn_permlane32_swap(__float_as_uint(v), __float_as_uint(v), false, false); return vmax(__uint_as_float(a[0]), __uint_as_float(a[1])); }

#define PLD(i) "s_mov_b64 exec, %[m" #i "]\n\ts_cbranch_execz .Lpoll%=_" #i "\n\tglobal_load_dwordx4 %[g" #i "], %[vo], %[b] sc1\n.Lpoll%=_" #i ":\n\t"
#define PADV "s_mov_b64 exec, %[sv]\n\tv_add_u32 %[vo], %[st], %[vo]\n\t"
template <int STRIDE>
__device__ __forceinline__ void repoll3(const char* base, u32 off, const u64 (&m)[3], v4u (&g)[3]) {
    u64 sv;
    asm volatile("s_mov_b64 %[sv], exec\n\t" PLD(0) PADV PLD(1) PADV PLD(2) "s_mov_b64 exec, %[sv]\n\ts_waitcnt vmcnt(0)"
                 : [sv] "=&s"(sv), [vo] "+v"(off), [g0] "+v"(g[0]), [g1] "+v"(g[1]), [g2] "+v"(g[2])
                 : [m0] "s"(m[0]), [m1] "s"(m[1]), [m2] "s"(m[2]), [b] "s"(base), [st] "i"(STRIDE) : "memory");
}
template <int STRIDE>
__device__ __forceinline__ void repoll8(const char* base, u32 off, const u64 (&m)[8], v4u (&g)[8]) {
    u64 sv;
    asm volatile("s_mov_b64 %[sv], exec\n\t" PLD(0) PADV PLD(1) PADV PLD(2) PADV PLD(3) PADV PLD(4) PADV PLD(5) PADV PLD(6) PADV PLD(7) "s_mov_b64 exec, %[sv]\n\ts_waitcnt vmcnt(0)"
                 : [sv] "=&s"(sv), [vo] "+v"(off), [g0] "+v"(g[0]), [g1] "+v"(g[1]), [g2] "+v"(g[2]), [g3] "+v"(g[3]), [g4] "+v"(g[4]), [g5] "+v"(g[5]), [g6] "+v"(g[6]), [g7] "+v"(g[7])
                 : [m0] "s"(m[0]), [m1] "s"(m[1]), [m2] "s"(m[2]), [m3] "s"(m[3]), [m4] "s"(m[4]), [m5] "s"(m[5]), [m6] "s"(m[6]), [m7] "s"(m[7]), [b] "s"(base), [st] "i"(STRIDE) : "memory");
}
__device__ __forceinline__ void glds16(const char* base, u32 off, u32 lds_base) {
    u32 sv; asm volatile("s_mov_b32 %0, m0\n\ts_mov_b32 m0, %3\n\ts_nop 0\n\tglobal_load_lds_dwordx4 %1, %2\n\ts_mov_b32 m0, %0" : "=&s"(sv) : "v"(off), "s"(base), "s"(lds_base) : "memory");
}

__global__ __launch_bounds__(T, 2) void cbam_kernel(const char* x, const char* w1, const char* w2, char* out, char* ws) {
    __shared__ __attribute__((aligned(16))) char lds[L_END];
    const int t = threadIdx.x, lane = t & 63;
    const int w = __builtin_amdgcn_readfirstlane(t >> 6);
    const int b = blockIdx.x;
    const int slice = (b >> 3) & 1, rg = (b & 7) | ((b >> 4) << 3);
    const bool isred = b < NRED;
    asm volatile("" : "+s"(x), "+s"(w1), "+s"(w2), "+s"(out), "+s"(ws));

    const size_t rbase = (size_t)rg * RGB;
    const __amdgpu_buffer_rsrc_t rx = __builtin_amdgcn_make_buffer_rsrc((void*)(x + rbase + slice * 384), 0, RGB - slice * 384, 0x00020000);
    const __amdgpu_buffer_rsrc_t ro = __builtin_amdgcn_make_buffer_rsrc((void*)(out + rbase + slice * 384), 0, RGB - slice * 384, 0x00020000);
    const __amdgpu_buffer_rsrc_t rws = __builtin_amdgcn_make_buffer_rsrc((void*)ws, 0, WS_END, 0x00020000);
    const __amdgpu_buffer_rsrc_t rw1 = __builtin_amdgcn_make_buffer_rsrc((void*)w1, 0, C * HID * 2, 0x00020000);

    const int hrow = lane >> 5, j = lane & 31;
    const u32 voff = (u32)((2 * w + hrow) * ROWB + 12 * j);

    v32u X[4];
    v4u wr1 = {0u, 0u, 0u, 0u};
#pragma unroll
    for (int k = 0; k < 32; ++k) {
        const v3u ld = __builtin_amdgcn_raw_buffer_load_b96(rx, voff, k * KSTEP, AUX_NT);
        X[k >> 3][4 * (k & 7)] = ld[0]; X[k >> 3][4 * (k & 7) + 1] = ld[1]; X[k >> 3][4 * (k & 7) + 2] = ld[2];
        if (k % GRP == GRP - 1 && k != 31) { __builtin_amdgcn_sched_barrier(0); asm volatile("s_barrier" ::: "memory"); __builtin_amdgcn_sched_barrier(0); }
        if (k == 7) { if (isred && t < 384) wr1 = __builtin_amdgcn_raw_buffer_load_b128(rw1, (u32)(b * 6144 + t * 16), 0, 0); __builtin_amdgcn_sched_barrier(0); }
    }
    u64 epoch;
    asm volatile("s_load_dwordx2 %0, %1, 0x0\n\ts_waitcnt lgkmcnt(0)" : "=s"(epoch) : "s"(ws) : "memory");
    u64 tag = epoch * 6364136223846793005ull + 1442695040888963407ull;
    asm volatile("" : "+s"(tag));
    const u32 tagLo = (u32)tag, tagHi = (u32)(tag >> 32);

    const int q = lane >> 4, p4 = lane & 3;
    const int trow = 4 * q + ((lane & 15) >> 2);
    u32 a_part = (u32)(L_RS + (w * SC + 6 * j + 3 * hrow) * 4);
    u32 a_sum = (u32)(L_RS + t * 4);
    u32 a_mb1 = (u32)(WS_MB1 + (((slice * 6 + (t >> 5)) * NRG + rg) * 32 + (t & 31)) * 16);
    u32 a_mb2 = (u32)(WS_MB2 + (q * HID + 16 * w + (lane & 15)) * 16);
    u32 a_hw; { const int n = 16 * w + (lane & 15), kk = n & 31;
                a_hw = (u32)(L_H + ((lane & 16) ? 320 : 0) + ((n & ~31) + 8 * ((kk & 15) >> 2) + 4 * (kk >> 4) + (kk & 3)) * 2); }
    u32 a_hr = (u32)(L_H + (lane & 1) * 320 + q * 16);
    u32 a_scw = (u32)(L_SC + (32 * w + (lane & 31)) * 4), a_scr = (u32)(L_SC + 24 * j);
    asm volatile("" : "+v"(a_part), "+v"(a_sum), "+v"(a_mb1), "+v"(a_mb2), "+v"(a_hw), "+v"(a_hr), "+v"(a_scw), "+v"(a_scr));

    v2f s[3]; float m[6];
#pragma unroll
    for (int i = 0; i < 3; ++i) { s[i] = (v2f){0.f, 0.f}; m[2 * i] = -INFINITY; m[2 * i + 1] = -INFINITY; }
#pragma unroll
    for (int k = 0; k < 32; k += 2) {
#pragma unroll
        for (int i = 0; i < 3; ++i) {
            const u32 da = X[k >> 3][4 * (k & 7) + i], dc = X[k >> 3][4 * (k & 7) + 4 + i];
            const v2f a = {bflo(da), bfhi(da)}, c = {bflo(dc), bfhi(dc)};
            s[i] += a; s[i] += c;
            m[2 * i] = max3(m[2 * i], a[0], c[0]); m[2 * i + 1] = max3(m[2 * i + 1], a[1], c[1]);
        }
        asm volatile("" : "+v"(s[0]), "+v"(s[1]), "+v"(s[2]), "+v"(m[0]), "+v"(m[1]), "+v"(m[2]), "+v"(m[3]), "+v"(m[4]), "+v"(m[5]));
        if (k == 16 && isred && t < 384) {
            const int row = t / 12, c = t - row * 12;
            *(LAS v4u*)(LAS char*)(lds + L_W1 + row * P1 + (c ^ (((row >> 2) & 1) << 1)) * 16) = wr1;
        }
    }
    {
        LAS float* rs = (LAS float*)(LAS char*)(lds + a_part);
        LAS float* rm = (LAS float*)(LAS char*)(lds + a_part + (L_RM - L_RS));
#pragma unroll
        for (int i = 0; i < 3; ++i) {
            auto a = __builtin_amdgcn_permlane32_swap(__float_as_uint(s[i >> 1][i & 1]), __float_as_uint(s[(i + 3) >> 1][(i + 3) & 1]), false, false);
            auto c = __builtin_amdgcn_permlane32_swap(__float_as_uint(m[i]), __float_as_uint(m[i + 3]), false, false);
            rs[i] = __uint_as_float(a[0]) + __uint_as_float(a[1]);
            rm[i] = vmax(__uint_as_float(c[0]), __uint_as_float(c[1]));
        }
    }
    __syncthreads();
    if (t < SC) {
        const LAS float* rs = (const LAS float*)(LAS char*)(lds + a_sum);
        const LAS float* rm = (const LAS float*)(LAS char*)(lds + a_sum + (L_RM - L_RS));
        float a[8], c[8];
#pragma unroll
        for (int i = 0; i < 8; ++i) { a[i] = rs[i * SC]; c[i] = rm[i * SC]; }
        asm volatile("" : "+v"(a[0]), "+v"(a[1]), "+v"(a[2]), "+v"(a[3]), "+v"(a[4]), "+v"(a[5]), "+v"(a[6]), "+v"(a[7]),
                          "+v"(c[0]), "+v"(c[1]), "+v"(c[2]), "+v"(c[3]), "+v"(c[4]), "+v"(c[5]), "+v"(c[6]), "+v"(c[7]));
        const float ss = ((a[0] + a[1]) + (a[2] + a[3])) + ((a[4] + a[5]) + (a[6] + a[7]));
        const float mm = vmax8(c[0], c[1], c[2], c[3], c[4], c[5], c[6], c[7]);
        const v4u g = {__float_as_uint(ss), tagLo, __float_as_uint(mm), tagHi};
        __builtin_amdgcn_raw_buffer_store_b128(g, rws, a_mb1, 0, AUX_SC1);
    }

    if (isred) {
        s16x8 f1;
        {
            const int wt = w < 6 ? w : 5;
            const LAS char* b1 = (const LAS char*)(lds + L_W1 + trow * P1 + ((wt ^ ((trow >> 2) & 1)) * 32) + p4 * 8);
            const s16x4 lo = __builtin_amdgcn_ds_read_tr16_b64_v4i16((LAS s16x4*)b1);
            const s16x4 hi = __builtin_amdgcn_ds_read_tr16_b64_v4i16((LAS s16x4*)(b1 + 16 * P1));
            f1 = __builtin_shufflevector(lo, hi, 0, 1, 2, 3, 4, 5, 6, 7);
        }
        const u32 off = (u32)(WS_MB1 + b * (NRG * 32 * 16) + t * 16);
        u32 a_rrw = (u32)(L_RR + (w * 32 + j) * 8), a_rrr = (u32)(L_RR + (hrow * 128 + j) * 8);
        u32 a_gaw = (u32)(L_GA + w * 192 + hrow * 96 + (8 * ((j & 15) >> 2) + 4 * (j >> 4) + (j & 3)) * 2), a_gar = (u32)(L_GA + w * 192 + (lane & 1) * 96 + q * 16);
        u32 a_pub = (u32)(WS_MB2 + (b * HID + 16 * w + (lane & 15)) * 16);
        asm volatile("" : "+v"(a_rrw), "+v"(a_rrr), "+v"(a_gaw), "+v"(a_gar), "+v"(a_pub), "+v"(f1));
        v4u g[8]; u64 miss[8]; u32 spins = 0;
#pragma unroll
        for (int i = 0; i < 8; ++i) { g[i] = (v4u){0u, ~tagLo, 0u, 0u}; miss[i] = ~0ull; }
#pragma clang loop unroll(disable)
        for (;;) {
            repoll8<8192>(ws, off, miss, g);
            u64 any = 0;
#pragma unroll
            for (int i = 0; i < 8; ++i) { miss[i] = __ballot(g[i][1] != tagLo) | __ballot(g[i][3] != tagHi); any |= miss[i]; }
            if (any == 0 || ++spins > kSpinCap) break;
        }
        float ss = ((__uint_as_float(g[0][0]) + __uint_as_float(g[1][0])) + (__uint_as_float(g[2][0]) + __uint_as_float(g[3][0]))) +
                   ((__uint_as_float(g[4][0]) + __uint_as_float(g[5][0])) + (__uint_as_float(g[6][0]) + __uint_as_float(g[7][0])));
        float mm = vmax8(__uint_as_float(g[0][2]), __uint_as_float(g[1][2]), __uint_as_float(g[2][2]), __uint_as_float(g[3][2]),
                         __uint_as_float(g[4][2]), __uint_as_float(g[5][2]), __uint_as_float(g[6][2]), __uint_as_float(g[7][2]));
        ss = add32(ss); mm = max32(mm);
        if (lane < 32) *(LAS v2f*)(LAS char*)(lds + a_rrw) = (v2f){ss, mm};
        __syncthreads();
        if (w < 6) {
            const LAS v2f* pr = (const LAS v2f*)(LAS char*)(lds + a_rrr);
            const v2f v0 = pr[0], v1 = pr[32], v2 = pr[64], v3 = pr[96];
            ss = add32((v0[0] + v1[0]) + (v2[0] + v3[0]));
            mm = max32(vmax(vmax3(v0[1], v1[1], v2[1]), v3[1]));
            const float val = hrow ? mm : ss * (1.0f / NPOS);
            *(LAS unsigned short*)(LAS char*)(lds + a_gaw) = (unsigned short)(cvtpk(val, val) & 0xffffu);
            const s16x8 a = *(const LAS s16x8*)(LAS char*)(lds + a_gar);
            v4f acc = {0.f, 0.f, 0.f, 0.f};
            acc = __builtin_amdgcn_mfma_f32_16x16x32_bf16(a, f1, acc, 0, 0, 0);
            const v4u g2 = {__float_as_uint(acc[0]), tagLo, __float_as_uint(acc[1]), tagHi};
            if (lane < 16) __builtin_amdgcn_raw_buffer_store_b128(g2, rws, a_pub, 0, AUX_SC1);
        }
    }

    {
        const u32 lbase = (u32)(uintptr_t)(LAS char*)lds;
#pragma unroll
        for (int mm = 0; mm < 5; ++mm) {
            if (mm < 4 || w < 4) {
                const int i = t + 512 * mm, row = i / 24, pc = i - row * 24;
                const int c = pc ^ (((row >> 1) & 3) << 1);
                glds16(w2, (u32)(row * ROWB + slice * 384 + c * 16), lbase + L_W2 + (w * 64 + 512 * mm) * 16);
            }
        }
    }
#pragma unroll
    for (int g = 0; g < 4; ++g) asm volatile("" : "+v"(X[g]));
    asm volatile("s_waitcnt vmcnt(0)" ::: "memory");
    __syncthreads();
    s16x8 f2[6];
    {
        const int wt = w < 6 ? w : 5;
#pragma unroll
        for (int tl = 0; tl < 2; ++tl) {
            const LAS char* b2 = (const LAS char*)(lds + L_W2 + trow * P2 + (((2 * wt + tl) ^ ((trow >> 1) & 3)) * 32) + p4 * 8);
#pragma unroll
            for (int st = 0; st < 3; ++st) {
                const s16x4 lo = __builtin_amdgcn_ds_read_tr16_b64_v4i16((LAS s16x4*)(b2 + (32 * st) * P2));
                const s16x4 hi = __builtin_amdgcn_ds_read_tr16_b64_v4i16((LAS s16x4*)(b2 + (32 * st + 16) * P2));
                f2[tl * 3 + st] = __builtin_shufflevector(lo, hi, 0, 1, 2, 3, 4, 5, 6, 7);
            }
        }
#pragma unroll
        for (int st = 0; st < 6; ++st) asm volatile("" : "+v"(f2[st]));
    }

    v4u g[3]; u64 miss[3];
#pragma unroll
    for (int i = 0; i < 3; ++i) { g[i] = (v4u){0u, ~tagLo, 0u, 0u}; miss[i] = ~0ull; asm volatile("" : "+v"(g[i])); }
    if (w == 0) {
        const u32 so = (u32)(WS_MB2 + ((b % NRED) * HID + 80) * 16);
        u32 left = kSpinCap;
        asm volatile("" : "+s"(left));
        for (;;) {
            v4u sg; asm volatile("global_load_dwordx4 %0, %1, %2 sc1\n\ts_waitcnt vmcnt(0)" : "=&v"(sg) : "v"(so), "s"(ws) : "memory");
            const u64 got = ((u64)(u32)__builtin_amdgcn_readfirstlane(sg[3]) << 32) | (u32)__builtin_amdgcn_readfirstlane(sg[1]);
            if (got == tag || --left == 0) break;
            __builtin_amdgcn_s_sleep(1);
        }
    }
    __builtin_amdgcn_s_barrier();
    if (w < 6) {
        u32 spins = 0;
#pragma clang loop unroll(disable)
        for (;;) {
            repoll3<4 * HID * 16>(ws, a_mb2, miss, g);
            u64 any = 0;
#pragma unroll
            for (int i = 0; i < 3; ++i) { miss[i] = __ballot(g[i][1] != tagLo) | __ballot(g[i][3] != tagHi); any |= miss[i]; }
            if (any == 0 || ++spins > kSpinCap) break;
            __builtin_amdgcn_s_sleep(2);
        }
        float ha = (__uint_as_float(g[0][0]) + __uint_as_float(g[1][0])) + __uint_as_float(g[2][0]);
        float hm = (__uint_as_float(g[0][2]) + __uint_as_float(g[1][2])) + __uint_as_float(g[2][2]);
        ha = add32(add16(ha)); hm = add32(add16(hm));
        float hv = rbf((lane & 16) ? hm : ha);
        hv = hv * (hv > 0.f ? 1.f : 0.f);
        if (lane < 32) *(LAS unsigned short*)(LAS char*)(lds + a_hw) = (unsigned short)(cvtpk(hv, hv) & 0xffffu);
    }
    __syncthreads();
    if (w < 6) {
        const LAS char* ha = (const LAS char*)(lds + a_hr);
        v4f a0 = {0.f, 0.f, 0.f, 0.f}, a1 = {0.f, 0.f, 0.f, 0.f};
        s16x8 ah[3];
#pragma unroll
        for (int st = 0; st < 3; ++st) ah[st] = *(const LAS s16x8*)(ha + st * 64);
        __builtin_amdgcn_sched_barrier(0);
#pragma unroll
        for (int st = 0; st < 3; ++st) {
            a0 = __builtin_amdgcn_mfma_f32_16x16x32_bf16(ah[st], f2[st], a0, 0, 0, 0);
            a1 = __builtin_amdgcn_mfma_f32_16x16x32_bf16(ah[st], f2[3 + st], a1, 0, 0, 0);
        }
        const float za = (lane & 16) ? a1[0] : a0[0], zm = (lane & 16) ? a1[1] : a0[1];
        const float z = rbf(rbf(za) + rbf(zm));
        const float sg = rbf(__builtin_amdgcn_rcpf(1.0f + __expf(-z)));
        if (lane < 32) *(LAS float*)(LAS char*)(lds + a_scw) = sg;
    }
    __syncthreads();

    v2f sc[3];
    {
        const LAS v2f* sp = (const LAS v2f*)(LAS char*)(lds + a_scr);
#pragma unroll
        for (int i = 0; i < 3; ++i) sc[i] = sp[i];
    }
    const u64 t0 = now();
#pragma unroll
    for (int g4 = 0; g4 < 4; ++g4) {
#pragma unroll 1
        for (int i = 0; i < 8; ++i) {
            if ((i & 3) == 0 && (g4 | i)) { const u64 due = t0 + (u64)(((8 * g4 + i) * PACE) >> 2); while (now() < due) __builtin_amdgcn_s_sleep(1); }
            const u32 d0 = X[g4][4 * i], d1 = X[g4][4 * i + 1], d2 = X[g4][4 * i + 2];
            v3u o;
            { const v2f v = (v2f){bflo(d0), bfhi(d0)} * sc[0]; o[0] = cvtpk(v[0], v[1]); }
            { const v2f v = (v2f){bflo(d1), bfhi(d1)} * sc[1]; o[1] = cvtpk(v[0], v[1]); }
            { const v2f v = (v2f){bflo(d2), bfhi(d2)} * sc[2]; o[2] = cvtpk(v[0], v[1]); }
            __builtin_amdgcn_raw_buffer_store_b96(o, ro, voff, (g4 * 8 + i) * KSTEP, AUX_SC1);
        }
    }
    if (b == 0 && t == 0) __hip_atomic_store((u64*)ws, tag, __ATOMIC_RELAXED, __HIP_MEMORY_SCOPE_AGENT);
}

extern "C" void kernel_launch(void* const* d_in, const int* in_sizes, int n_in,
                              void* d_out, int out_size, void* d_ws, size_t ws_size,
                              hipStream_t stream) {
    cbam_kernel<<<NWG, T, 0, stream>>>((const char*)d_in[0], (const char*)d_in[1], (const char*)d_in[2], (char*)d_out, (char*)d_ws);
}
```

```cpp
#include <hip/hip_runtime.h>
#include <hip/hip_bf16.h>
#include <stdint.h>

typedef unsigned u32;
typedef unsigned long long u64;
typedef u32 v3u __attribute__((ext_vector_type(3)));
typedef u32 v4u __attribute__((ext_vector_type(4)));
typedef u32 v32u __attribute__((ext_vector_type(32)));
typedef float v2f __attribute__((ext_vector_type(2)));
typedef float v4f __attribute__((ext_vector_type(4)));
typedef short s16x4 __attribute__((ext_vector_type(4)));
typedef short s16x8 __attribute__((ext_vector_type(8)));
#define LAS __attribute__((address_space(3)))

static constexpr int C = 384, HID = 96, NPOS = 65536;
static constexpr int NWG = 256, T = 512;
static constexpr int SC = 192;
static constexpr int NRG = 128;
static constexpr int ROWB = 768;
static constexpr int RGB = 512 * ROWB;
static constexpr int KSTEP = 16 * ROWB;
static constexpr int NRED = 12;
static constexpr int AUX_NT = 2, AUX_SC1 = 16;
static constexpr int GRP = 8;
static constexpr int PACE = 75;
static constexpr u32 WS_MB2 = 4096;
static constexpr u32 WS_MB1 = 4096 + 20480;
static constexpr u32 WS_END = WS_MB1 + NRED * NRG * 32 * 16;
static constexpr int P1 = 192, P2 = 384;
static constexpr int L_W2 = 0;
static constexpr int L_W1 = L_W2 + HID * P2;
static constexpr int L_RS = L_W1 + 32 * P1;
static constexpr int L_RM = L_RS + 8 * SC * 4;
static constexpr int L_H = L_RM + 8 * SC * 4;
static constexpr int L_SC = L_H + 640;
static constexpr int L_RR = L_SC + SC * 4;
static constexpr int L_GA = L_RR + 8 * 32 * 8;
static constexpr int L_USED = L_GA + 6 * 192;
static constexpr int L_END = 84 * 1024;
static_assert(L_USED <= L_END, "lds map");
static constexpr u32 kSpinCap = 1u << 18;

__device__ __forceinline__ float bflo(u32 u) { return __uint_as_float(u << 16); }
__device__ __forceinline__ float bfhi(u32 u) { return __uint_as_float(u & 0xffff0000u); }
__device__ __forceinline__ u32 cvtpk(float lo, float hi) {
    typedef __bf16 bf2 __attribute__((ext_vector_type(2)));
    v2f v = {lo, hi};
    return __builtin_bit_cast(u32, __builtin_convertvector(v, bf2));
}
__device__ __forceinline__ float rbf(float f) { return bflo(cvtpk(f, f)); }
__device__ __forceinline__ float max3(float a, float b, float c) { return __builtin_fmaxf(__builtin_fmaxf(a, b), c); }
__device__ __forceinline__ float vmax(float a, float b) { float r; asm("v_max_f32 %0, %1, %2" : "=v"(r) : "v"(a), "v"(b)); return r; }
__device__ __forceinline__ float vmax3(float a, float b, float c) { float r; asm("v_max3_f32 %0, %1, %2, %3" : "=v"(r) : "v"(a), "v"(b), "v"(c)); return r; }
__device__ __forceinline__ float vmax8(float a, float b, float c, float d, float e, float f, float g, float h) { return vmax(vmax3(d, e, f), vmax3(g, h, vmax3(a, b, c))); }
__device__ __forceinline__ u64 now() { return __builtin_amdgcn_s_memrealtime(); }
__device__ __forceinline__ float add16(float v) { auto a = __builtin_amdgcn_permlane16_swap(__float_as_uint(v), __float_as_uint(v), false, false); return __uint_as_float(a[0]) + __uint_as_float(a[1]); }
__device__ __forceinline__ float add32(float v) { auto a = __builtin_amdgcn_permlane32_swap(__float_as_uint(v), __float_as_uint(v), false, false); return __uint_as_float(a[0]) + __uint_as_float(a[1]); }
__device__ __forceinline__ float max32(float v) { auto a = __builtin_amdgcn_permlane32_swap(__float_as_uint(v), __float_as_uint(v), false, false); return vmax(__uint_as_float(a[0]), __uint_as_float(a[1])); }

#define PLD(i) "s_mov_b64 exec, %[m" #i "]\n\ts_cbranch_execz .Lpoll%=_" #i "\n\tglobal_load_dwordx4 %[g" #i "], %[vo], %[b] offset:%c[st]*" #i " sc1\n.Lpoll%=_" #i ":\n\t"
template <int STRIDE>
__device__ __forceinline__ void repoll3(const char* base, u32 off, const u64 (&m)[3], v4u (&g)[3]) {
    static_assert(2 * STRIDE < 4096, "immediate offset");
    u64 sv;
    asm volatile("s_mov_b64 %[sv], exec\n\t" PLD(0) PLD(1) PLD(2) "s_mov_b64 exec, %[sv]\n\ts_waitcnt vmcnt(0)"
                 : [sv] "=&s"(sv), [g0] "+v"(g[0]), [g1] "+v"(g[1]), [g2] "+v"(g[2])
                 : [m0] "s"(m[0]), [m1] "s"(m[1]), [m2] "s"(m[2]), [vo] "v"(off), [b] "s"(base), [st] "i"(STRIDE) : "memory");
}
template <int STRIDE>
__device__ __forceinline__ void repoll8(const char* base, u32 off, const u64 (&m)[8], v4u (&g)[8]) {
    static_assert(7 * STRIDE < 4096, "immediate offset");
    u64 sv;
    asm volatile("s_mov_b64 %[sv], exec\n\t" PLD(0) PLD(1) PLD(2) PLD(3) PLD(4) PLD(5) PLD(6) PLD(7) "s_mov_b64 exec, %[sv]\n\ts_waitcnt vmcnt(0)"
                 : [sv] "=&s"(sv), [g0] "+v"(g[0]), [g1] "+v"(g[1]), [g2] "+v"(g[2]), [g3] "+v"(g[3]), [g4] "+v"(g[4]), [g5] "+v"(g[5]), [g6] "+v"(g[6]), [g7] "+v"(g[7])
                 : [m0] "s"(m[0]), [m1] "s"(m[1]), [m2] "s"(m[2]), [m3] "s"(m[3]), [m4] "s"(m[4]), [m5] "s"(m[5]), [m6] "s"(m[6]), [m7] "s"(m[7]), [vo] "v"(off), [b] "s"(base), [st] "i"(STRIDE) : "memory");
}
__device__ __forceinline__ void glds16(const char* base, u32 off, u32 lds_base) {
    u32 sv; asm volatile("s_mov_b32 %0, m0\n\ts_mov_b32 m0, %3\n\ts_nop 0\n\tglobal_load_lds_dwordx4 %1, %2\n\ts_mov_b32 m0, %0" : "=&s"(sv) : "v"(off), "s"(base), "s"(lds_base) : "memory");
}

__global__ __launch_bounds__(T, 2) void cbam_kernel(const char* x, const char* w1, const char* w2, char* out, char* ws) {
    __shared__ __attribute__((aligned(16))) char lds[L_END];
    const int t = threadIdx.x, lane = t & 63;
    const int w = __builtin_amdgcn_readfirstlane(t >> 6);
    const int b = blockIdx.x;
    const int slice = (b >> 3) & 1, rg = (b & 7) | ((b >> 4) << 3);
    const bool isred = b < NRED;
    asm volatile("" : "+s"(x), "+s"(w1), "+s"(w2), "+s"(out), "+s"(ws));

    const size_t rbase = (size_t)rg * RGB;
    const __amdgpu_buffer_rsrc_t rx = __builtin_amdgcn_make_buffer_rsrc((void*)(x + rbase + slice * 384), 0, RGB - slice * 384, 0x00020000);
    const __amdgpu_buffer_rsrc_t ro = __builtin_amdgcn_make_buffer_rsrc((void*)(out + rbase + slice * 384), 0, RGB - slice * 384, 0x00020000);
    const __amdgpu_buffer_rsrc_t rws = __builtin_amdgcn_make_buffer_rsrc((void*)ws, 0, WS_END, 0x00020000);
    const __amdgpu_buffer_rsrc_t rw1 = __builtin_amdgcn_make_buffer_rsrc((void*)w1, 0, C * HID * 2, 0x00020000);

    const int hrow = lane >> 5, j = lane & 31;
    const u32 voff = (u32)((2 * w + hrow) * ROWB + 12 * j);

    v32u X[4];
    v4u wr1 = {0u, 0u, 0u, 0u};
#pragma unroll
    for (int k = 0; k < 32; ++k) {
        const v3u ld = __builtin_amdgcn_raw_buffer_load_b96(rx, voff, k * KSTEP, AUX_NT);
        X[k >> 3][4 * (k & 7)] = ld[0]; X[k >> 3][4 * (k & 7) + 1] = ld[1]; X[k >> 3][4 * (k & 7) + 2] = ld[2];
        if (k % GRP == GRP - 1 && k != 31) { __builtin_amdgcn_sched_barrier(0); asm volatile("s_barrier" ::: "memory"); __builtin_amdgcn_sched_barrier(0); }
        if (k == 7) { if (isred && t < 384) wr1 = __builtin_amdgcn_raw_buffer_load_b128(rw1, (u32)(b * 6144 + t * 16), 0, 0); __builtin_amdgcn_sched_barrier(0); }
    }
    u64 epoch;
    asm volatile("s_load_dwordx2 %0, %1, 0x0\n\ts_waitcnt lgkmcnt(0)" : "=s"(epoch) : "s"(ws) : "memory");
    u64 tag = epoch * 6364136223846793005ull + 1442695040888963407ull;
    asm volatile("" : "+s"(tag));
    const u32 tagLo = (u32)tag, tagHi = (u32)(tag >> 32);

    const int q = lane >> 4, p4 = lane & 3;
    const int trow = 4 * q + ((lane & 15) >> 2);
    u32 a_part = (u32)(L_RS + (w * SC + 6 * j + 3 * hrow) * 4);
    u32 a_sum = (u32)(L_RS + t * 4);
    u32 a_mb1 = (u32)(WS_MB1 + (((slice * 6 + (t >> 5)) * NRG + rg) * 32 + (t & 31)) * 16);
    u32 a_mb2 = (u32)(WS_MB2 + (3 * q * HID + 16 * w + (lane & 15)) * 16);
    u32 a_hw; { const int n = 16 * w + (lane & 15), kk = n & 31;
                a_hw = (u32)(L_H + ((lane & 16) ? 320 : 0) + ((n & ~31) + 8 * ((kk & 15) >> 2) + 4 * (kk >> 4) + (kk & 3)) * 2); }
    u32 a_hr = (u32)(L_H + (lane & 1) * 320 + q * 16);
    u32 a_scw = (u32)(L_SC + (32 * w + (lane & 31)) * 4), a_scr = (u32)(L_SC + 24 * j);
    asm volatile("" : "+v"(a_part), "+v"(a_sum), "+v"(a_mb1), "+v"(a_mb2), "+v"(a_hw), "+v"(a_hr), "+v"(a_scw), "+v"(a_scr));

    v2f s[3]; float m[6];
#pragma unroll
    for (int i = 0; i < 3; ++i) { s[i] = (v2f){0.f, 0.f}; m[2 * i] = -INFINITY; m[2 * i + 1] = -INFINITY; }
#pragma unroll
    for (int k = 0; k < 32; k += 2) {
#pragma unroll
        for (int i = 0; i < 3; ++i) {
            const u32 da = X[k >> 3][4 * (k & 7) + i], dc = X[k >> 3][4 * (k & 7) + 4 + i];
            const v2f a = {bflo(da), bfhi(da)}, c = {bflo(dc), bfhi(dc)};
            s[i] += a; s[i] += c;
            m[2 * i] = max3(m[2 * i], a[0], c[0]); m[2 * i + 1] = max3(m[2 * i + 1], a[1], c[1]);
        }
        asm volatile("" : "+v"(s[0]), "+v"(s[1]), "+v"(s[2]), "+v"(m[0]), "+v"(m[1]), "+v"(m[2]), "+v"(m[3]), "+v"(m[4]), "+v"(m[5]));
        if (k == 16 && isred && t < 384) {
            const int row = t / 12, c = t - row * 12;
            *(LAS v4u*)(LAS char*)(lds + L_W1 + row * P1 + (c ^ (((row >> 2) & 1) << 1)) * 16) = wr1;
        }
    }
    {
        LAS float* rs = (LAS float*)(LAS char*)(lds + a_part);
        LAS float* rm = (LAS float*)(LAS char*)(lds + a_part + (L_RM - L_RS));
#pragma unroll
        for (int i = 0; i < 3; ++i) {
            auto a = __builtin_amdgcn_permlane32_swap(__float_as_uint(s[i >> 1][i & 1]), __float_as_uint(s[(i + 3) >> 1][(i + 3) & 1]), false, false);
            auto c = __builtin_amdgcn_permlane32_swap(__float_as_uint(m[i]), __float_as_uint(m[i + 3]), false, false);
            rs[i] = __uint_as_float(a[0]) + __uint_as_float(a[1]);
            rm[i] = vmax(__uint_as_float(c[0]), __uint_as_float(c[1]));
        }
    }
    __syncthreads();
    if (t < SC) {
        const LAS float* rs = (const LAS float*)(LAS char*)(lds + a_sum);
        const LAS float* rm = (const LAS float*)(LAS char*)(lds + a_sum + (L_RM - L_RS));
        float a[8], c[8];
#pragma unroll
        for (int i = 0; i < 8; ++i) { a[i] = rs[i * SC]; c[i] = rm[i * SC]; }
        asm volatile("" : "+v"(a[0]), "+v"(a[1]), "+v"(a[2]), "+v"(a[3]), "+v"(a[4]), "+v"(a[5]), "+v"(a[6]), "+v"(a[7]),
                          "+v"(c[0]), "+v"(c[1]), "+v"(c[2]), "+v"(c[3]), "+v"(c[4]), "+v"(c[5]), "+v"(c[6]), "+v"(c[7]));
        const float ss = ((a[0] + a[1]) + (a[2] + a[3])) + ((a[4] + a[5]) + (a[6] + a[7]));
        const float mm = vmax8(c[0], c[1], c[2], c[3], c[4], c[5], c[6], c[7]);
        const v4u g = {__float_as_uint(ss), tagLo, __float_as_uint(mm), tagHi};
        __builtin_amdgcn_raw_buffer_store_b128(g, rws, a_mb1, 0, AUX_SC1);
    }

    if (isred) {
        s16x8 f1;
        {
            const int wt = w < 6 ? w : 5;
            const LAS char* b1 = (const LAS char*)(lds + L_W1 + trow * P1 + ((wt ^ ((trow >> 2) & 1)) * 32) + p4 * 8);
            const s16x4 lo = __builtin_amdgcn_ds_read_tr16_b64_v4i16((LAS s16x4*)b1);
            const s16x4 hi = __builtin_amdgcn_ds_read_tr16_b64_v4i16((LAS s16x4*)(b1 + 16 * P1));
            f1 = __builtin_shufflevector(lo, hi, 0, 1, 2, 3, 4, 5, 6, 7);
        }
        const u32 off = (u32)(WS_MB1 + b * (NRG * 32 * 16) + (t >> 5) * 4096 + (t & 31) * 16);
        u32 a_rrw = (u32)(L_RR + (w * 32 + j) * 8), a_rrr = (u32)(L_RR + (hrow * 128 + j) * 8);
        u32 a_gaw = (u32)(L_GA + w * 192 + hrow * 96 + (8 * ((j & 15) >> 2) + 4 * (j >> 4) + (j & 3)) * 2), a_gar = (u32)(L_GA + w * 192 + (lane & 1) * 96 + q * 16);
        u32 a_pub = (u32)(WS_MB2 + (b * HID + 16 * w + (lane & 15)) * 16);
        asm volatile("" : "+v"(a_rrw), "+v"(a_rrr), "+v"(a_gaw), "+v"(a_gar), "+v"(a_pub), "+v"(f1));
        v4u g[8]; u64 miss[8]; u32 spins = 0;
#pragma unroll
        for (int i = 0; i < 8; ++i) { g[i] = (v4u){0u, ~tagLo, 0u, 0u}; miss[i] = ~0ull; }
#pragma clang loop unroll(disable)
        for (;;) {
            repoll8<512>(ws, off, miss, g);
            u64 any = 0;
#pragma unroll
            for (int i = 0; i < 8; ++i) { miss[i] = __ballot(g[i][1] != tagLo) | __ballot(g[i][3] != tagHi); any |= miss[i]; }
            if (any == 0 || ++spins > kSpinCap) break;
        }
        float ss = ((__uint_as_float(g[0][0]) + __uint_as_float(g[1][0])) + (__uint_as_float(g[2][0]) + __uint_as_float(g[3][0]))) +
                   ((__uint_as_float(g[4][0]) + __uint_as_float(g[5][0])) + (__uint_as_float(g[6][0]) + __uint_as_float(g[7][0])));
        float mm = vmax8(__uint_as_float(g[0][2]), __uint_as_float(g[1][2]), __uint_as_float(g[2][2]), __uint_as_float(g[3][2]),
                         __uint_as_float(g[4][2]), __uint_as_float(g[5][2]), __uint_as_float(g[6][2]), __uint_as_float(g[7][2]));
        ss = add32(ss); mm = max32(mm);
        if (lane < 32) *(LAS v2f*)(LAS char*)(lds + a_rrw) = (v2f){ss, mm};
        __syncthreads();
        if (w < 6) {
            const LAS v2f* pr = (const LAS v2f*)(LAS char*)(lds + a_rrr);
            const v2f v0 = pr[0], v1 = pr[32], v2 = pr[64], v3 = pr[96];
            ss = add32((v0[0] + v1[0]) + (v2[0] + v3[0]));
            mm = max32(vmax(vmax3(v0[1], v1[1], v2[1]), v3[1]));
            const float val = hrow ? mm : ss * (1.0f / NPOS);
            *(LAS unsigned short*)(LAS char*)(lds + a_gaw) = (unsigned short)(cvtpk(val, val) & 0xffffu);
            const s16x8 a = *(const LAS s16x8*)(LAS char*)(lds + a_gar);
            v4f acc = {0.f, 0.f, 0.f, 0.f};
            acc = __builtin_amdgcn_mfma_f32_16x16x32_bf16(a, f1, acc, 0, 0, 0);
            const v4u g2 = {__float_as_uint(acc[0]), tagLo, __float_as_uint(acc[1]), tagHi};
            if (lane < 16) __builtin_amdgcn_raw_buffer_store_b128(g2, rws, a_pub, 0, AUX_SC1);
        }
    }

    {
        const u32 lbase = (u32)(uintptr_t)(LAS char*)lds;
#pragma unroll
        for (int mm = 0; mm < 5; ++mm) {
            if (mm < 4 || w < 4) {
                const int i = t + 512 * mm, row = i / 24, pc = i - row * 24;
                const int c = pc ^ (((row >> 1) & 3) << 1);
                glds16(w2, (u32)(row * ROWB + slice * 384 + c * 16), lbase + L_W2 + (w * 64 + 512 * mm) * 16);
            }
        }
    }
#pragma unroll
    for (int g = 0; g < 4; ++g) asm volatile("" : "+v"(X[g]));
    asm volatile("s_waitcnt vmcnt(0)" ::: "memory");
    __syncthreads();
    s16x8 f2[6];
    {
        const int wt = w < 6 ? w : 5;
#pragma unroll
        for (int tl = 0; tl < 2; ++tl) {
            const LAS char* b2 = (const LAS char*)(lds + L_W2 + trow * P2 + (((2 * wt + tl) ^ ((trow >> 1) & 3)) * 32) + p4 * 8);
#pragma unroll
            for (int st = 0; st < 3; ++st) {
                const s16x4 lo = __builtin_amdgcn_ds_read_tr16_b64_v4i16((LAS s16x4*)(b2 + (32 * st) * P2));
                const s16x4 hi = __builtin_amdgcn_ds_read_tr16_b64_v4i16((LAS s16x4*)(b2 + (32 * st + 16) * P2));
                f2[tl * 3 + st] = __builtin_shufflevector(lo, hi, 0, 1, 2, 3, 4, 5, 6, 7);
            }
        }
#pragma unroll
        for (int st = 0; st < 6; ++st) asm volatile("" : "+v"(f2[st]));
    }

    v4u g[3]; u64 miss[3];
#pragma unroll
    for (int i = 0; i < 3; ++i) { g[i] = (v4u){0u, ~tagLo, 0u, 0u}; miss[i] = ~0ull; asm volatile("" : "+v"(g[i])); }
    if (w == 0) {
        const u32 so = (u32)(WS_MB2 + ((b % NRED) * HID + 80) * 16);
        u32 left = kSpinCap;
        asm volatile("" : "+s"(left));
        for (;;) {
            v4u sg; asm volatile("global_load_dwordx4 %0, %1, %2 sc1\n\ts_waitcnt vmcnt(0)" : "=&v"(sg) : "v"(so), "s"(ws) : "memory");
            const u64 got = ((u64)(u32)__builtin_amdgcn_readfirstlane(sg[3]) << 32) | (u32)__builtin_amdgcn_readfirstlane(sg[1]);
            if (got == tag || --left == 0) break;
            __builtin_amdgcn_s_sleep(1);
        }
    }
    __builtin_amdgcn_s_barrier();
    if (w < 6) {
        u32 spins = 0;
#pragma clang loop unroll(disable)
        for (;;) {
            repoll3<HID * 16>(ws, a_mb2, miss, g);
            u64 any = 0;
#pragma unroll
            for (int i = 0; i < 3; ++i) { miss[i] = __ballot(g[i][1] != tagLo) | __ballot(g[i][3] != tagHi); any |= miss[i]; }
            if (any == 0 || ++spins > kSpinCap) break;
            __builtin_amdgcn_s_sleep(2);
        }
        float ha = (__uint_as_float(g[0][0]) + __uint_as_float(g[1][0])) + __uint_as_float(g[2][0]);
        float hm = (__uint_as_float(g[0][2]) + __uint_as_float(g[1][2])) + __uint_as_float(g[2][2]);
        ha = add32(add16(ha)); hm = add32(add16(hm));
        float hv = rbf((lane & 16) ? hm : ha);
        hv = hv * (hv > 0.f ? 1.f : 0.f);
        if (lane < 32) *(LAS unsigned short*)(LAS char*)(lds + a_hw) = (unsigned short)(cvtpk(hv, hv) & 0xffffu);
    }
    __syncthreads();
    if (w < 6) {
        const LAS char* ha = (const LAS char*)(lds + a_hr);
        v4f a0 = {0.f, 0.f, 0.f, 0.f}, a1 = {0.f, 0.f, 0.f, 0.f};
        s16x8 ah[3];
#pragma unroll
        for (int st = 0; st < 3; ++st) ah[st] = *(const LAS s16x8*)(ha + st * 64);
        __builtin_amdgcn_sched_barrier(0);
#pragma unroll
        for (int st = 0; st < 3; ++st) {
            a0 = __builtin_amdgcn_mfma_f32_16x16x32_bf16(ah[st], f2[st], a0, 0, 0, 0);
            a1 = __builtin_amdgcn_mfma_f32_16x16x32_bf16(ah[st], f2[3 + st], a1, 0, 0, 0);
        }
        const float za = (lane & 16) ? a1[0] : a0[0], zm = (lane & 16) ? a1[1] : a0[1];
        const float z = rbf(rbf(za) + rbf(zm));
        const float sg = rbf(__builtin_amdgcn_rcpf(1.0f + __expf(-z)));
        if (lane < 32) *(LAS float*)(LAS char*)(lds + a_scw) = sg;
    }
    __syncthreads();

    v2f sc[3];
    {
        const LAS v2f* sp = (const LAS v2f*)(LAS char*)(lds + a_scr);
#pragma unroll
        for (int i = 0; i < 3; ++i) sc[i] = sp[i];
    }
    const u64 t0 = now();
#pragma unroll
    for (int g4 = 0; g4 < 4; ++g4) {
#pragma unroll 1
        for (int i = 0; i < 8; ++i) {
            if ((i & 3) == 0 && (g4 | i)) { const u64 due = t0 + (u64)(((8 * g4 + i) * PACE) >> 2); while (now() < due) __builtin_amdgcn_s_sleep(1); }
            const u32 d0 = X[g4][4 * i], d1 = X[g4][4 * i + 1], d2 = X[g4][4 * i + 2];
            v3u o;
            { const v2f v = (v2f){bflo(d0), bfhi(d0)} * sc[0]; o[0] = cvtpk(v[0], v[1]); }
            { const v2f v = (v2f){bflo(d1), bfhi(d1)} * sc[1]; o[1] = cvtpk(v[0], v[1]); }
            { const v2f v = (v2f){bflo(d2), bfhi(d2)} * sc[2]; o[2] = cvtpk(v[0], v[1]); }
            __builtin_amdgcn_raw_buffer_store_b96(o, ro, voff, (g4 * 8 + i) * KSTEP, AUX_SC1);
        }
    }
    if (b == 0 && t == 0) __hip_atomic_store((__attribute__((address_space(1))) u64*)ws, tag, __ATOMIC_RELAXED, __HIP_MEMORY_SCOPE_AGENT);
}

extern "C" void kernel_launch(void* const* d_in, const int* in_sizes, int n_in,
                              void* d_out, int out_size, void* d_ws, size_t ws_size,
                              hipStream_t stream) {
    cbam_kernel<<<NWG, T, 0, stream>>>((const char*)d_in[0], (const char*)d_in[1], (const char*)d_in[2], (char*)d_out, (char*)d_ws);
}
```
